```python
import torch

def _softmax(x, axis=-1):
    return torch.softmax(x, dim=axis)
def _gelu(x):
    return torch.nn.functional.gelu(x, approximate="tanh")
def _silu(x):
    return torch.nn.functional.silu(x)
def _sigmoid(x):
    return torch.sigmoid(x)
def _layernorm(x, axis=-1, eps=1e-5):
    m = x.mean(dim=axis, keepdim=True)
    v = x.var(dim=axis, keepdim=True, unbiased=False)
    return (x - m) / torch.sqrt(v + eps)
def _scatter_add(tgt, idx, src, axis=0):
    return tgt.clone().index_add_(axis, idx, src)
def _segment_sum(src, seg, n):
    out = torch.zeros((n,) + src.shape[1:], dtype=src.dtype)
    return out.index_add_(0, seg, src)
def _pad(x, pw):
    flat = tuple(v for pair in reversed(pw) for v in pair)
    return torch.nn.functional.pad(x, flat)

kernel_name = 'recurrent_scan__gqa_block_cached__glu_gated_geglu__decode'

def setup_inputs():

    _w = (lambda a: (lambda t: t.to(torch.bfloat16) if t.is_floating_point() else t)(torch.as_tensor(a)))
    def _A_setup():
        B, Sq, D = 1, 1, 576
        Hq, Hkv, Sk, Dh = 32, 16, 65536, 128
        assert Hq % Hkv == 0, (Hq, Hkv)
        return {
            "x": _w((lambda *s: torch.randn(*s, dtype=torch.bfloat16) * 0.4)(B, Sq, D)),
            "k_cache": _w((lambda *s: torch.randn(*s, dtype=torch.bfloat16) * 1.6)(B, Hkv, Sk, Dh)),
            "v_cache": _w((lambda *s: torch.randn(*s, dtype=torch.bfloat16) * 1.6)(B, Hkv, Sk, Dh)),
            "wq": _w((lambda *s: torch.randn(*s, dtype=torch.bfloat16) * 1.6)(D, Hq * Dh) * (D ** -0.5)),
            "wo": _w((lambda *s: torch.randn(*s, dtype=torch.bfloat16) * 1.6)(Hq * Dh, D) * ((Hq * Dh) ** -0.5)),
        }
    _A_in = _A_setup()

    def _B_setup():
        B, S, Dm, Df = 1, 1, 576, 4608
        return {
            "x": _w((lambda *s: torch.randn(*s, dtype=torch.bfloat16) * 0.4)(B, S, Dm)),
            "w_gate": _w((lambda *s: torch.randn(*s, dtype=torch.bfloat16) * 1.6)(Dm, Df) * (Dm ** -0.5)),
            "w_up": _w((lambda *s: torch.randn(*s, dtype=torch.bfloat16) * 1.6)(Dm, Df) * (Dm ** -0.5)),
            "w_down": _w((lambda *s: torch.randn(*s, dtype=torch.bfloat16) * 1.6)(Df, Dm) * (Df ** -0.5)),
        }
    _B_in = _B_setup()

    return {
        "A_x": _A_in["x"],
        "A_k_cache": _A_in["k_cache"],
        "A_v_cache": _A_in["v_cache"],
        "A_wq": _A_in["wq"],
        "A_wo": _A_in["wo"],
        "B_x": _B_in["x"],
        "B_w_gate": _B_in["w_gate"],
        "B_w_up": _B_in["w_up"],
        "B_w_down": _B_in["w_down"],
    }

def reference(A_x, A_k_cache, A_v_cache, A_wq, A_wo, B_x, B_w_gate, B_w_up, B_w_down):
    def _A_ref(x, k_cache, v_cache, wq, wo):
        B, Sq, D = x.shape
        Hq, Dh = 32, 128
        Hkv = k_cache.shape[1]
        rep = Hq // Hkv
        q = (lambda x, *d: x.permute(*d))((x @ wq).reshape(B, Sq, Hq, Dh), 0, 2, 1, 3)
        k = (lambda x, n, axis: torch.repeat_interleave(x, n, dim=axis))(k_cache, rep, axis=1)
        v = (lambda x, n, axis: torch.repeat_interleave(x, n, dim=axis))(v_cache, rep, axis=1)
        scores = torch.einsum("bhqd,bhkd->bhqk", q, k) * (Dh ** -0.5)
        p = _softmax(scores, axis=-1)
        o = torch.einsum("bhqk,bhkd->bhqd", p, v)
        return (lambda x, *d: x.permute(*d))(o, 0, 2, 1, 3).reshape(B, Sq, Hq * Dh) @ wo

    def _B_ref(x, w_gate, w_up, w_down):
        g = torch.einsum("bsd,df->bsf", x, w_gate)
        u = torch.einsum("bsd,df->bsf", x, w_up)
        h = _gelu(g) * u
        return torch.einsum("bsf,fd->bsd", h, w_down)

    x = A_x
    xn = _layernorm(x, axis=-1)
    s = _A_ref(xn, A_k_cache, A_v_cache, A_wq, A_wo)
    g = _silu(_B_ref(xn, B_w_gate, B_w_up, B_w_down))
    return x + s * g
```

```cpp
#include <hip/hip_runtime.h>
#include <hip/hip_bf16.h>
#include <stdint.h>

typedef __attribute__((ext_vector_type(4))) unsigned u32x4;
typedef __attribute__((ext_vector_type(4))) float f32x4;
typedef __attribute__((ext_vector_type(2))) __bf16 bf16x2;

constexpr int DM = 576, HQ = 32, HKV = 16, SK = 65536, DH = 128, DF = 4608, NQ = HQ * DH;
constexpr int NSPLIT = 32, WAVES = 4, KPS = SK / NSPLIT;
constexpr int PPW = KPS / 16 / WAVES;

constexpr size_t WS_Q = 0;
constexpr size_t WS_GLU = 16384;
constexpr size_t WS_PML = WS_GLU + 72 * 576 * 4 + 4096;
constexpr size_t WS_PO = WS_PML + HQ * NSPLIT * 2 * 4;
constexpr size_t WS_Y = WS_PO + (size_t)HQ * NSPLIT * DH * 4;

__device__ __forceinline__ float bf2f(unsigned short v) { return __uint_as_float(((unsigned)v) << 16); }
__device__ __forceinline__ float bflo(unsigned v) { return __uint_as_float(v << 16); }
__device__ __forceinline__ float bfhi(unsigned v) { return __uint_as_float(v & 0xffff0000u); }
__device__ __forceinline__ float rbf(float x) { return __bfloat162float(__float2bfloat16(x)); }
__device__ __forceinline__ float dot2(unsigned k, unsigned q, float acc) {
  return __builtin_amdgcn_fdot2_f32_bf16(__builtin_bit_cast(bf16x2, k), __builtin_bit_cast(bf16x2, q), acc, false);
}
template <int CTRL> __device__ __forceinline__ float dpp(float x) {
  return __builtin_bit_cast(float, __builtin_amdgcn_mov_dpp(__builtin_bit_cast(int, x), CTRL, 0xf, 0xf, true));
}
constexpr int XOR1 = 0xB1, XOR2 = 0x4E, XOR7 = 0x141, XOR8 = 0x128, ROR4 = 0x124;
__device__ __forceinline__ float xrow16_max(float x) {
  auto s = __builtin_amdgcn_permlane16_swap(__float_as_uint(x), __float_as_uint(x), false, false);
  x = fmaxf(__uint_as_float(s[0]), __uint_as_float(s[1]));
  auto t = __builtin_amdgcn_permlane32_swap(__float_as_uint(x), __float_as_uint(x), false, false);
  return fmaxf(__uint_as_float(t[0]), __uint_as_float(t[1]));
}
__device__ __forceinline__ float xrow16_sum(float x) {
  auto s = __builtin_amdgcn_permlane16_swap(__float_as_uint(x), __float_as_uint(x), false, false);
  x = __uint_as_float(s[0]) + __uint_as_float(s[1]);
  auto t = __builtin_amdgcn_permlane32_swap(__float_as_uint(x), __float_as_uint(x), false, false);
  return __uint_as_float(t[0]) + __uint_as_float(t[1]);
}

__device__ __forceinline__ void layernorm_lds(const unsigned short* x, float* xs, float* red) {
  const int t = threadIdx.x;
  float v0 = 0.f, v1 = 0.f, v2 = 0.f;
  v0 = bf2f(x[t]); v1 = bf2f(x[t + 256]); if (t < 64) v2 = bf2f(x[t + 512]);
  float s = v0 + v1 + v2;
  for (int o = 32; o > 0; o >>= 1) s += __shfl_xor(s, o);
  if ((t & 63) == 0) red[t >> 6] = s;
  __syncthreads();
  float mean = (red[0] + red[1] + red[2] + red[3]) * (1.f / DM);
  __syncthreads();
  float d0 = v0 - mean, d1 = v1 - mean, d2 = (t < 64) ? v2 - mean : 0.f;
  float q = d0 * d0 + d1 * d1 + d2 * d2;
  for (int o = 32; o > 0; o >>= 1) q += __shfl_xor(q, o);
  if ((t & 63) == 0) red[t >> 6] = q;
  __syncthreads();
  float var = (red[0] + red[1] + red[2] + red[3]) * (1.f / DM);
  float rs = 1.0f / sqrtf(var + 1e-5f);
  xs[t] = rbf(d0 * rs); xs[t + 256] = rbf(d1 * rs); if (t < 64) xs[t + 512] = rbf(d2 * rs);
  __syncthreads();
}

__device__ __forceinline__ void gemv_chunk(const unsigned short* W, int ncol, int col0, const float* xs, float* part  ) {
  const int t = threadIdx.x, lane = t & 63, w = t >> 6, rg = lane >> 3, cc = lane & 7;
  float acc[8];
#pragma unroll
  for (int k = 0; k < 8; ++k) acc[k] = 0.f;
  const unsigned short* p = W + (size_t)(w * 144 + rg) * ncol + col0 + cc * 8;
#pragma unroll
  for (int i = 0; i < 18; ++i) {
    u32x4 v = *reinterpret_cast<const u32x4*>(p + (size_t)i * 8 * ncol);
    float xv = xs[w * 144 + i * 8 + rg];
#pragma unroll
    for (int k = 0; k < 4; ++k) { acc[2 * k] = fmaf(xv, bflo(v[k]), acc[2 * k]); acc[2 * k + 1] = fmaf(xv, bfhi(v[k]), acc[2 * k + 1]); }
  }
#pragma unroll
  for (int k = 0; k < 8; ++k) { float a = acc[k]; a += __shfl_xor(a, 8); a += __shfl_xor(a, 16); a += __shfl_xor(a, 32); acc[k] = a; }
  if (rg == 0) {
#pragma unroll
    for (int k = 0; k < 8; ++k) part[w * 64 + cc * 8 + k] = acc[k];
  }
}

__global__ void __launch_bounds__(256) k_prep(const unsigned short* x, const unsigned short* wq, const unsigned short* wg,
                                              const unsigned short* wu, const unsigned short* wd, unsigned char* ws) {
  __shared__ float xs[DM];
  __shared__ float red[4];
  __shared__ float pa[4 * 64], pb[4 * 64], hs[64];
  __shared__ float yred[3][576];
  const int t = threadIdx.x;
  layernorm_lds(x, xs, red);
  if (blockIdx.x < 64) {
    gemv_chunk(wq, NQ, blockIdx.x * 64, xs, pa);
    __syncthreads();
    if (t < 64) {
      float q = pa[t] + pa[64 + t] + pa[128 + t] + pa[192 + t];
      reinterpret_cast<__hip_bfloat16*>(ws + WS_Q)[blockIdx.x * 64 + t] = __float2bfloat16(q);
    }
  } else {
    const int ch = blockIdx.x - 64;
    gemv_chunk(wg, DF, ch * 64, xs, pa);
    gemv_chunk(wu, DF, ch * 64, xs, pb);
    __syncthreads();
    if (t < 64) {
      float g = rbf(pa[t] + pa[64 + t] + pa[128 + t] + pa[192 + t]);
      float u = rbf(pb[t] + pb[64 + t] + pb[128 + t] + pb[192 + t]);
      float ge = 0.5f * g * (1.f + tanhf(0.7978845608028654f * (g + 0.044715f * g * g * g)));
      hs[t] = rbf(rbf(ge) * u);
    }
    __syncthreads();
    if (t < 216) {
      const int c = t % 72, rg = t / 72;
      float acc[8];
#pragma unroll
      for (int k = 0; k < 8; ++k) acc[k] = 0.f;
      for (int r = rg; r < 64; r += 3) {
        u32x4 v = *reinterpret_cast<const u32x4*>(wd + (size_t)(ch * 64 + r) * DM + c * 8);
        float hv = hs[r];
#pragma unroll
        for (int k = 0; k < 4; ++k) { acc[2 * k] = fmaf(hv, bflo(v[k]), acc[2 * k]); acc[2 * k + 1] = fmaf(hv, bfhi(v[k]), acc[2 * k + 1]); }
      }
#pragma unroll
      for (int k = 0; k < 8; ++k) yred[rg][c * 8 + k] = acc[k];
    }
    __syncthreads();
    float* yo = reinterpret_cast<float*>(ws + WS_GLU) + ch * 576;
    for (int i = t; i < 576; i += 256) yo[i] = yred[0][i] + yred[1][i] + yred[2][i];
  }
}

__global__ void __launch_bounds__(256) k_attn(const unsigned short* kc, const unsigned short* vc, unsigned char* ws) {
  __shared__ __attribute__((aligned(16))) float lds[WAVES * 64 + WAVES * 2 * 132];
  const int kvh = blockIdx.x & 15, s = blockIdx.x >> 4;
  const int tid = threadIdx.x, lane = tid & 63, wave = __builtin_amdgcn_readfirstlane(tid >> 6), r = lane >> 4, c = lane & 15;
  constexpr float C2 = 0.088388347648318440f * 1.4426950408889634f;
  const unsigned short* qb = reinterpret_cast<const unsigned short*>(ws + WS_Q) + kvh * 256 + 8 * c;
  u32x4 q0 = *reinterpret_cast<const u32x4*>(qb), q1 = *reinterpret_cast<const u32x4*>(qb + 128);
  const size_t base = ((size_t)kvh * SK + (size_t)s * KPS + (size_t)wave * 16 + r) * DH + 8 * c;
  const unsigned short* kp = kc + base;
  const unsigned short* vp = vc + base;
  u32x4 Kn[4], Vn[4];
  float m = -INFINITY, l = 0.f, acc[16];
#pragma unroll
  for (int k = 0; k < 16; ++k) acc[k] = 0.f;
#define ISSUE(i_) do { _Pragma("unroll") for (int t_ = 0; t_ < 4; ++t_) { \
    Kn[t_] = __builtin_nontemporal_load(reinterpret_cast<const u32x4*>(kp + ((size_t)(i_) * 64 + t_ * 4) * DH)); \
    Vn[t_] = __builtin_nontemporal_load(reinterpret_cast<const u32x4*>(vp + ((size_t)(i_) * 64 + t_ * 4) * DH)); } } while (0)
  ISSUE(0);
  for (int i = 0; i < PPW; ++i) {
    u32x4 Kc[4], Vc[4];
#pragma unroll
    for (int t = 0; t < 4; ++t) { Kc[t] = Kn[t]; Vc[t] = Vn[t]; }
    if (i + 1 < PPW) ISSUE(i + 1);
    float x[8];
#pragma unroll
    for (int t = 0; t < 4; ++t) {
      float d0 = 0.f, d1 = 0.f;
#pragma unroll
      for (int k = 0; k < 4; ++k) { d0 = dot2(Kc[t][k], q0[k], d0); d1 = dot2(Kc[t][k], q1[k], d1); }
      x[2 * t] = d0; x[2 * t + 1] = d1;
    }
#define TR_STEP(HALF, CTRL, BIT) _Pragma("unroll") for (int h = 0; h < (HALF); ++h) { \
      const float lo_ = x[h], hi_ = x[h + (HALF)]; const float t1_ = lo_ + dpp<CTRL>(lo_), t2_ = hi_ + dpp<CTRL>(hi_); \
      x[h] = (c & (BIT)) ? t2_ : t1_; }
    TR_STEP(4, XOR8, 8) TR_STEP(2, XOR7, 4) TR_STEP(1, XOR2, 2)
#undef TR_STEP
    float sc = x[0] + dpp<XOR1>(x[0]);
    sc *= C2;
    float pm = sc; pm = fmaxf(pm, dpp<ROR4>(pm)); pm = fmaxf(pm, dpp<XOR8>(pm)); pm = xrow16_max(pm);
    const float mn = fmaxf(m, pm);
    const float alpha = __builtin_amdgcn_exp2f(m - mn), p = __builtin_amdgcn_exp2f(sc - mn);
    l = fmaf(l, alpha, p); m = mn;
    const float a0 = dpp<0x00>(alpha), a1 = dpp<0xAA>(alpha);
#pragma unroll
    for (int k = 0; k < 8; ++k) { acc[k] *= a0; acc[8 + k] *= a1; }
    float* prow = lds + (wave * 4 + r) * 16;
    prow[c] = p;
    asm volatile("s_waitcnt lgkmcnt(0)" ::: "memory");
    f32x4 pr[4];
#pragma unroll
    for (int u = 0; u < 4; ++u) pr[u] = *reinterpret_cast<const f32x4*>(prow + 4 * u);
#pragma unroll
    for (int t = 0; t < 4; ++t) {
      const float p0 = pr[t][0], p1 = pr[t][2];
#pragma unroll
      for (int k = 0; k < 4; ++k) {
        const float lo = bflo(Vc[t][k]), hi = bfhi(Vc[t][k]);
        acc[2 * k] = fmaf(p0, lo, acc[2 * k]); acc[2 * k + 1] = fmaf(p0, hi, acc[2 * k + 1]);
        acc[8 + 2 * k] = fmaf(p1, lo, acc[8 + 2 * k]); acc[8 + 2 * k + 1] = fmaf(p1, hi, acc[8 + 2 * k + 1]);
      }
    }
  }
#undef ISSUE
#pragma unroll
  for (int k = 0; k < 16; ++k) acc[k] = xrow16_sum(acc[k]);
  { float t = l; t += dpp<ROR4>(t); t += dpp<XOR8>(t); l = xrow16_sum(t); }
  float* mrg = lds + WAVES * 64;
  if (r == 0) {
#pragma unroll
    for (int g = 0; g < 2; ++g) {
      float* rec = mrg + (wave * 2 + g) * 132;
#pragma unroll
      for (int k = 0; k < 8; k += 4) *reinterpret_cast<f32x4*>(rec + 4 + 8 * c + k) = f32x4{acc[g * 8 + k], acc[g * 8 + k + 1], acc[g * 8 + k + 2], acc[g * 8 + k + 3]};
      if (c == 2 * g) { rec[0] = m; rec[1] = l; }
    }
  }
  __syncthreads();
  if (wave < 2) {
    const int g = wave, d0 = 2 * lane;
    float M = -INFINITY;
#pragma unroll
    for (int w = 0; w < WAVES; ++w) M = fmaxf(M, mrg[(w * 2 + g) * 132]);
    float L = 0.f, o0 = 0.f, o1 = 0.f;
#pragma unroll
    for (int w = 0; w < WAVES; ++w) { const float* rec = mrg + (w * 2 + g) * 132;
      const float wt = __builtin_amdgcn_exp2f(rec[0] - M);
      L = fmaf(wt, rec[1], L); o0 = fmaf(wt, rec[4 + d0], o0); o1 = fmaf(wt, rec[4 + d0 + 1], o1); }
    const int head = kvh * 2 + g;
    float* pml = reinterpret_cast<float*>(ws + WS_PML) + ((size_t)head * NSPLIT + s) * 2;
    float* po = reinterpret_cast<float*>(ws + WS_PO) + ((size_t)head * NSPLIT + s) * DH;
    *reinterpret_cast<float2*>(po + d0) = make_float2(o0, o1);
    if (lane == 0) { pml[0] = M; pml[1] = L; }
  }
}

__global__ void __launch_bounds__(256) k_comb(const unsigned short* wo, unsigned char* ws) {
  __shared__ float os[128];
  __shared__ float yred[3][576];
  const int head = blockIdx.x, t = threadIdx.x;
  const float* pml = reinterpret_cast<const float*>(ws + WS_PML) + (size_t)head * NSPLIT * 2;
  const float* po = reinterpret_cast<const float*>(ws + WS_PO) + (size_t)head * NSPLIT * DH;
  if (t < 128) {
    float M = -INFINITY;
    for (int s = 0; s < NSPLIT; ++s) M = fmaxf(M, pml[2 * s]);
    float L = 0.f, o = 0.f;
    for (int s = 0; s < NSPLIT; ++s) { float w = exp2f(pml[2 * s] - M); L = fmaf(w, pml[2 * s + 1], L); o = fmaf(w, po[s * DH + t], o); }
    os[t] = rbf(o / L);
  }
  __syncthreads();
  if (t < 216) {
    const int c = t % 72, rg = t / 72;
    float acc[8];
#pragma unroll
    for (int k = 0; k < 8; ++k) acc[k] = 0.f;
    for (int r = rg; r < 128; r += 3) {
      u32x4 v = *reinterpret_cast<const u32x4*>(wo + (size_t)(head * 128 + r) * DM + c * 8);
      float hv = os[r];
#pragma unroll
      for (int k = 0; k < 4; ++k) { acc[2 * k] = fmaf(hv, bflo(v[k]), acc[2 * k]); acc[2 * k + 1] = fmaf(hv, bfhi(v[k]), acc[2 * k + 1]); }
    }
#pragma unroll
    for (int k = 0; k < 8; ++k) yred[rg][c * 8 + k] = acc[k];
  }
  __syncthreads();
  float* yo = reinterpret_cast<float*>(ws + WS_Y) + head * 576;
  for (int i = t; i < 576; i += 256) yo[i] = yred[0][i] + yred[1][i] + yred[2][i];
}

__global__ void __launch_bounds__(576) k_final(const unsigned short* x, const unsigned char* ws, __hip_bfloat16* out) {
  const int t = threadIdx.x;
  const float* y = reinterpret_cast<const float*>(ws + WS_Y);
  const float* gl = reinterpret_cast<const float*>(ws + WS_GLU);
  float s = 0.f, g = 0.f;
  for (int h = 0; h < 32; ++h) s += y[h * 576 + t];
  for (int h = 0; h < 72; ++h) g += gl[h * 576 + t];
  s = rbf(s); g = rbf(g);
  float sg = rbf(g / (1.f + __expf(-g)));
  out[t] = __float2bfloat16(bf2f(x[t]) + rbf(s * sg));
}

extern "C" void kernel_launch(void* const* d_in, const int* in_sizes, int n_in, void* d_out, int out_size, void* d_ws, size_t ws_size,
                              hipStream_t stream) {
  const unsigned short* x = (const unsigned short*)d_in[0];
  const unsigned short* kc = (const unsigned short*)d_in[1];
  const unsigned short* vc = (const unsigned short*)d_in[2];
  const unsigned short* wq = (const unsigned short*)d_in[3];
  const unsigned short* wo = (const unsigned short*)d_in[4];
  const unsigned short* wg = (const unsigned short*)d_in[6];
  const unsigned short* wu = (const unsigned short*)d_in[7];
  const unsigned short* wd = (const unsigned short*)d_in[8];
  unsigned char* ws = (unsigned char*)d_ws;
  k_prep<<<136, 256, 0, stream>>>(x, wq, wg, wu, wd, ws);
  k_attn<<<HKV * NSPLIT, 256, 0, stream>>>(kc, vc, ws);
  k_comb<<<32, 256, 0, stream>>>(wo, ws);
  k_final<<<1, 576, 0, stream>>>(x, ws, (__hip_bfloat16*)d_out);
}
```

```cpp
#include <hip/hip_runtime.h>
#include <hip/hip_bf16.h>
#include <stdint.h>

typedef __attribute__((ext_vector_type(4))) unsigned u32x4;
typedef __attribute__((ext_vector_type(4))) float f32x4;
typedef __attribute__((ext_vector_type(2))) __bf16 bf16x2;

constexpr int DM = 576, HQ = 32, HKV = 16, SK = 65536, DH = 128, DF = 4608, NQ = HQ * DH;
constexpr int NSPLIT = 16, WAVES = 8, KPS = SK / NSPLIT;
constexpr int PPW = KPS / 16 / WAVES;
constexpr int NATT = HKV * NSPLIT;
constexpr int NGLU = 72;

constexpr size_t WS_CNT = 0;
constexpr size_t WS_GLU = 4096;
constexpr size_t WS_PML = WS_GLU + 72 * 576 * 4;
constexpr size_t WS_PO = WS_PML + HQ * NSPLIT * 2 * 4;
constexpr size_t WS_Y = WS_PO + (size_t)HQ * NSPLIT * DH * 4;

__device__ __forceinline__ float bf2f(unsigned short v) { return __uint_as_float(((unsigned)v) << 16); }
__device__ __forceinline__ float bflo(unsigned v) { return __uint_as_float(v << 16); }
__device__ __forceinline__ float bfhi(unsigned v) { return __uint_as_float(v & 0xffff0000u); }
__device__ __forceinline__ float rbf(float x) { return __bfloat162float(__float2bfloat16(x)); }
__device__ __forceinline__ float dot2(unsigned k, unsigned q, float acc) {
  return __builtin_amdgcn_fdot2_f32_bf16(__builtin_bit_cast(bf16x2, k), __builtin_bit_cast(bf16x2, q), acc, false);
}
template <int CTRL> __device__ __forceinline__ float dpp(float x) {
  return __builtin_bit_cast(float, __builtin_amdgcn_mov_dpp(__builtin_bit_cast(int, x), CTRL, 0xf, 0xf, true));
}
constexpr int XOR1 = 0xB1, XOR2 = 0x4E, XOR7 = 0x141, XOR8 = 0x128, ROR4 = 0x124;
__device__ __forceinline__ float xrow16_max(float x) {
  auto s = __builtin_amdgcn_permlane16_swap(__float_as_uint(x), __float_as_uint(x), false, false);
  x = fmaxf(__uint_as_float(s[0]), __uint_as_float(s[1]));
  auto t = __builtin_amdgcn_permlane32_swap(__float_as_uint(x), __float_as_uint(x), false, false);
  return fmaxf(__uint_as_float(t[0]), __uint_as_float(t[1]));
}
__device__ __forceinline__ float xrow16_sum(float x) {
  auto s = __builtin_amdgcn_permlane16_swap(__float_as_uint(x), __float_as_uint(x), false, false);
  x = __uint_as_float(s[0]) + __uint_as_float(s[1]);
  auto t = __builtin_amdgcn_permlane32_swap(__float_as_uint(x), __float_as_uint(x), false, false);
  return __uint_as_float(t[0]) + __uint_as_float(t[1]);
}
__device__ __forceinline__ float wave_sum(float s) {
#pragma unroll
  for (int o = 32; o > 0; o >>= 1) s += __shfl_xor(s, o);
  return s;
}

__device__ __forceinline__ void layernorm_wave(const unsigned short* x, float* xs, int lane) {
  float v[9];
#pragma unroll
  for (int j = 0; j < 9; ++j) v[j] = bf2f(x[lane + 64 * j]);
  float s = 0.f;
#pragma unroll
  for (int j = 0; j < 9; ++j) s += v[j];
  s = wave_sum(s);
  const float mean = s * (1.f / DM);
  float q = 0.f;
#pragma unroll
  for (int j = 0; j < 9; ++j) { const float d = v[j] - mean; q += d * d; }
  q = wave_sum(q);
  const float var = rbf(q * (1.f / DM));
  const float mb = rbf(mean);
  const float sd = rbf(sqrtf(rbf(var + 1e-5f)));
#pragma unroll
  for (int j = 0; j < 9; ++j) xs[lane + 64 * j] = rbf(rbf(v[j] - mb) / sd);
}

__global__ void __launch_bounds__(512, 4) k_main(const unsigned short* __restrict__ x, const unsigned short* __restrict__ kc,
                                                 const unsigned short* __restrict__ vc, const unsigned short* __restrict__ wq,
                                                 const unsigned short* __restrict__ wg, const unsigned short* __restrict__ wu,
                                                 const unsigned short* __restrict__ wd, unsigned char* __restrict__ ws) {
  __shared__ __attribute__((aligned(16))) float lds[576 + 8 * 256 + 8 * 64 + 8 * 2 * 132];
  float* xs = lds;
  float* part = lds + 576;
  float* pex = part + 8 * 256;
  float* mrg = pex + 8 * 64;
  const int tid = threadIdx.x, lane = tid & 63, wave = __builtin_amdgcn_readfirstlane(tid >> 6);

  if (blockIdx.x >= NATT) {
    const int ch = blockIdx.x - NATT;
    if (blockIdx.x == NATT && tid == 0) *reinterpret_cast<unsigned*>(ws + WS_CNT) = 0u;
    const int rg = lane >> 3, cc = lane & 7;
    u32x4 vg[9], vu[9];
    const size_t off = (size_t)(wave * 72 + rg) * DF + ch * 64 + cc * 8;
#pragma unroll
    for (int i = 0; i < 9; ++i) {
      vg[i] = __builtin_nontemporal_load(reinterpret_cast<const u32x4*>(wg + off + (size_t)i * 8 * DF));
      vu[i] = __builtin_nontemporal_load(reinterpret_cast<const u32x4*>(wu + off + (size_t)i * 8 * DF));
    }
    const int dc = tid % 72, dr = tid / 72;
    if (wave == 0) layernorm_wave(x, xs, lane);
    __syncthreads();
    float ag[8], au[8];
#pragma unroll
    for (int k = 0; k < 8; ++k) { ag[k] = 0.f; au[k] = 0.f; }
#pragma unroll
    for (int i = 0; i < 9; ++i) {
      const float xv = xs[wave * 72 + i * 8 + rg];
#pragma unroll
      for (int k = 0; k < 4; ++k) {
        ag[2 * k] = fmaf(xv, bflo(vg[i][k]), ag[2 * k]); ag[2 * k + 1] = fmaf(xv, bfhi(vg[i][k]), ag[2 * k + 1]);
        au[2 * k] = fmaf(xv, bflo(vu[i][k]), au[2 * k]); au[2 * k + 1] = fmaf(xv, bfhi(vu[i][k]), au[2 * k + 1]);
      }
    }
#pragma unroll
    for (int k = 0; k < 8; ++k) {
      float a = ag[k]; a += __shfl_xor(a, 8); a += __shfl_xor(a, 16); a += __shfl_xor(a, 32); ag[k] = a;
      float b = au[k]; b += __shfl_xor(b, 8); b += __shfl_xor(b, 16); b += __shfl_xor(b, 32); au[k] = b;
    }
    if (rg == 0) {
#pragma unroll
      for (int k = 0; k < 8; ++k) { part[wave * 128 + cc * 8 + k] = ag[k]; part[wave * 128 + 64 + cc * 8 + k] = au[k]; }
    }
    u32x4 vd[10];
    if (tid < 504) {
#pragma unroll
      for (int i = 0; i < 10; ++i) { const int r = dr + 7 * i; if (r < 64) vd[i] = __builtin_nontemporal_load(reinterpret_cast<const u32x4*>(wd + (size_t)(ch * 64 + r) * DM + dc * 8)); }
    }
    __syncthreads();
    float* hs = pex;
    if (tid < 64) {
      float g = 0.f, u = 0.f;
#pragma unroll
      for (int w = 0; w < 8; ++w) { g += part[w * 128 + tid]; u += part[w * 128 + 64 + tid]; }
      g = rbf(g); u = rbf(u);
      const float ge = 0.5f * g * (1.f + tanhf(0.7978845608028654f * (g + 0.044715f * g * g * g)));
      hs[tid] = rbf(rbf(ge) * u);
    }
    __syncthreads();
    float acc[8];
#pragma unroll
    for (int k = 0; k < 8; ++k) acc[k] = 0.f;
    if (tid < 504) {
#pragma unroll
      for (int i = 0; i < 10; ++i) { const int r = dr + 7 * i; if (r < 64) { const float hv = hs[r];
#pragma unroll
        for (int k = 0; k < 4; ++k) { acc[2 * k] = fmaf(hv, bflo(vd[i][k]), acc[2 * k]); acc[2 * k + 1] = fmaf(hv, bfhi(vd[i][k]), acc[2 * k + 1]); } } }
    }
    __syncthreads();
    float* yr = lds;
    if (tid < 504) {
#pragma unroll
      for (int k = 0; k < 8; ++k) yr[dr * 576 + dc * 8 + k] = acc[k];
    }
    __syncthreads();
    float* yo = reinterpret_cast<float*>(ws + WS_GLU) + ch * 576;
    for (int i = tid; i < 576; i += 512) {
      float s = 0.f;
#pragma unroll
      for (int r = 0; r < 7; ++r) s += yr[r * 576 + i];
      yo[i] = s;
    }
    return;
  }

  const int kvh = blockIdx.x & 15, sp = blockIdx.x >> 4;
  const int r = lane >> 4, c = lane & 15;
  const size_t base = ((size_t)kvh * SK + (size_t)sp * KPS + (size_t)wave * 16 + r) * DH + 8 * c;
  const unsigned short* kp = kc + base;
  const unsigned short* vp = vc + base;
  u32x4 Kn[4], Vn[4];
#define ISSUE(i_) do { _Pragma("unroll") for (int t_ = 0; t_ < 4; ++t_) { \
    Kn[t_] = __builtin_nontemporal_load(reinterpret_cast<const u32x4*>(kp + ((size_t)(i_) * (16 * WAVES) + t_ * 4) * DH)); \
    Vn[t_] = __builtin_nontemporal_load(reinterpret_cast<const u32x4*>(vp + ((size_t)(i_) * (16 * WAVES) + t_ * 4) * DH)); } } while (0)
  ISSUE(0);
  {
    const int hr = lane >> 5, cq = lane & 31;
    const unsigned short* wp = wq + (size_t)(wave * 72 + hr) * NQ + kvh * 256 + cq * 8;
    float aq[8];
#pragma unroll
    for (int k = 0; k < 8; ++k) aq[k] = 0.f;
    if (wave == 0) layernorm_wave(x, xs, lane);
    u32x4 wv[6];
#pragma unroll
    for (int i = 0; i < 6; ++i) wv[i] = *reinterpret_cast<const u32x4*>(wp + (size_t)i * 2 * NQ);
    __syncthreads();
#pragma unroll 1
    for (int b = 0; b < 6; ++b) {
      u32x4 cur[6];
#pragma unroll
      for (int i = 0; i < 6; ++i) cur[i] = wv[i];
      if (b < 5) {
#pragma unroll
        for (int i = 0; i < 6; ++i) wv[i] = *reinterpret_cast<const u32x4*>(wp + (size_t)((b + 1) * 6 + i) * 2 * NQ);
      }
#pragma unroll
      for (int i = 0; i < 6; ++i) {
        const float xv = xs[wave * 72 + (b * 6 + i) * 2 + hr];
#pragma unroll
        for (int k = 0; k < 4; ++k) { aq[2 * k] = fmaf(xv, bflo(cur[i][k]), aq[2 * k]); aq[2 * k + 1] = fmaf(xv, bfhi(cur[i][k]), aq[2 * k + 1]); }
      }
    }
#pragma unroll
    for (int k = 0; k < 8; ++k) aq[k] += __shfl_xor(aq[k], 32);
    if (hr == 0) {
      *reinterpret_cast<f32x4*>(part + wave * 256 + cq * 8) = f32x4{aq[0], aq[1], aq[2], aq[3]};
      *reinterpret_cast<f32x4*>(part + wave * 256 + cq * 8 + 4) = f32x4{aq[4], aq[5], aq[6], aq[7]};
    }
    __syncthreads();
  }
  u32x4 q0, q1;
  {
    float qa[8], qb[8];
#pragma unroll
    for (int k = 0; k < 8; ++k) { qa[k] = 0.f; qb[k] = 0.f; }
#pragma unroll
    for (int w = 0; w < 8; ++w) {
#pragma unroll
      for (int k = 0; k < 8; ++k) { qa[k] += part[w * 256 + 8 * c + k]; qb[k] += part[w * 256 + 128 + 8 * c + k]; }
    }
#pragma unroll
    for (int k = 0; k < 4; ++k) {
      q0[k] = (unsigned)__builtin_bit_cast(unsigned short, __float2bfloat16(qa[2 * k])) | ((unsigned)__builtin_bit_cast(unsigned short, __float2bfloat16(qa[2 * k + 1])) << 16);
      q1[k] = (unsigned)__builtin_bit_cast(unsigned short, __float2bfloat16(qb[2 * k])) | ((unsigned)__builtin_bit_cast(unsigned short, __float2bfloat16(qb[2 * k + 1])) << 16);
    }
  }
  constexpr float SC = 0.088388347648318440f, L2E = 1.4426950408889634f;
  float m = -INFINITY, l = 0.f, acc[16];
#pragma unroll
  for (int k = 0; k < 16; ++k) acc[k] = 0.f;
  for (int i = 0; i < PPW; ++i) {
    u32x4 Kc[4], Vc[4];
#pragma unroll
    for (int t = 0; t < 4; ++t) { Kc[t] = Kn[t]; Vc[t] = Vn[t]; }
    if (i + 1 < PPW) ISSUE(i + 1);
    float xx[8];
#pragma unroll
    for (int t = 0; t < 4; ++t) {
      float d0 = 0.f, d1 = 0.f;
#pragma unroll
      for (int k = 0; k < 4; ++k) { d0 = dot2(Kc[t][k], q0[k], d0); d1 = dot2(Kc[t][k], q1[k], d1); }
      xx[2 * t] = d0; xx[2 * t + 1] = d1;
    }
#define TR_STEP(HALF, CTRL, BIT) _Pragma("unroll") for (int h = 0; h < (HALF); ++h) { \
      const float lo_ = xx[h], hi_ = xx[h + (HALF)]; const float t1_ = lo_ + dpp<CTRL>(lo_), t2_ = hi_ + dpp<CTRL>(hi_); \
      xx[h] = (c & (BIT)) ? t2_ : t1_; }
    TR_STEP(4, XOR8, 8) TR_STEP(2, XOR7, 4) TR_STEP(1, XOR2, 2)
#undef TR_STEP
    float sc = xx[0] + dpp<XOR1>(xx[0]);
    sc = rbf(rbf(sc) * SC) * L2E;
    float pm = sc; pm = fmaxf(pm, dpp<ROR4>(pm)); pm = fmaxf(pm, dpp<XOR8>(pm)); pm = xrow16_max(pm);
    const float mn = fmaxf(m, pm);
    const float alpha = __builtin_amdgcn_exp2f(m - mn), p = __builtin_amdgcn_exp2f(sc - mn);
    l = fmaf(l, alpha, p); m = mn;
    const float a0 = dpp<0x00>(alpha), a1 = dpp<0xAA>(alpha);
#pragma unroll
    for (int k = 0; k < 8; ++k) { acc[k] *= a0; acc[8 + k] *= a1; }
    float* prow = pex + (wave * 4 + r) * 16;
    prow[c] = p;
    asm volatile("s_waitcnt lgkmcnt(0)" ::: "memory");
    f32x4 pr[4];
#pragma unroll
    for (int u = 0; u < 4; ++u) pr[u] = *reinterpret_cast<const f32x4*>(prow + 4 * u);
#pragma unroll
    for (int t = 0; t < 4; ++t) {
      const float p0 = pr[t][0], p1 = pr[t][2];
#pragma unroll
      for (int k = 0; k < 4; ++k) {
        const float lo = bflo(Vc[t][k]), hi = bfhi(Vc[t][k]);
        acc[2 * k] = fmaf(p0, lo, acc[2 * k]); acc[2 * k + 1] = fmaf(p0, hi, acc[2 * k + 1]);
        acc[8 + 2 * k] = fmaf(p1, lo, acc[8 + 2 * k]); acc[8 + 2 * k + 1] = fmaf(p1, hi, acc[8 + 2 * k + 1]);
      }
    }
  }
#undef ISSUE
#pragma unroll
  for (int k = 0; k < 16; ++k) acc[k] = xrow16_sum(acc[k]);
  { float t = l; t += dpp<ROR4>(t); t += dpp<XOR8>(t); l = xrow16_sum(t); }
  if (r == 0) {
#pragma unroll
    for (int g = 0; g < 2; ++g) {
      float* rec = mrg + (wave * 2 + g) * 132;
#pragma unroll
      for (int k = 0; k < 8; k += 4) *reinterpret_cast<f32x4*>(rec + 4 + 8 * c + k) = f32x4{acc[g * 8 + k], acc[g * 8 + k + 1], acc[g * 8 + k + 2], acc[g * 8 + k + 3]};
      if (c == 2 * g) { rec[0] = m; rec[1] = l; }
    }
  }
  __syncthreads();
  if (wave < 2) {
    const int g = wave, d0 = 2 * lane;
    float M = -INFINITY;
#pragma unroll
    for (int w = 0; w < WAVES; ++w) M = fmaxf(M, mrg[(w * 2 + g) * 132]);
    float L = 0.f, o0 = 0.f, o1 = 0.f;
#pragma unroll
    for (int w = 0; w < WAVES; ++w) { const float* rec = mrg + (w * 2 + g) * 132;
      const float wt = __builtin_amdgcn_exp2f(rec[0] - M);
      L = fmaf(wt, rec[1], L); o0 = fmaf(wt, rec[4 + d0], o0); o1 = fmaf(wt, rec[4 + d0 + 1], o1); }
    const int head = kvh * 2 + g;
    float* pml = reinterpret_cast<float*>(ws + WS_PML) + ((size_t)head * NSPLIT + sp) * 2;
    float* po = reinterpret_cast<float*>(ws + WS_PO) + ((size_t)head * NSPLIT + sp) * DH;
    *reinterpret_cast<float2*>(po + d0) = make_float2(o0, o1);
    if (lane == 0) { pml[0] = M; pml[1] = L; }
  }
}

__global__ void __launch_bounds__(576) k_final(const unsigned short* __restrict__ x, const unsigned short* __restrict__ wo,
                                               unsigned char* __restrict__ ws, __hip_bfloat16* __restrict__ out) {
  __shared__ float os[128];
  __shared__ float yred[8][576];
  __shared__ unsigned lastf;
  const int head = blockIdx.x, t = threadIdx.x;
  const int c = t % 72, rg = t / 72;
  u32x4 wv[16];
#pragma unroll
  for (int i = 0; i < 16; ++i) wv[i] = __builtin_nontemporal_load(reinterpret_cast<const u32x4*>(wo + (size_t)(head * 128 + rg * 16 + i) * DM + c * 8));
  const float* gl = reinterpret_cast<const float*>(ws + WS_GLU);
  float gsum = gl[head * 576 + t] + gl[(head + 32) * 576 + t] + (head < 8 ? gl[(head + 64) * 576 + t] : 0.f);
  const float* pml = reinterpret_cast<const float*>(ws + WS_PML) + (size_t)head * NSPLIT * 2;
  const float* po = reinterpret_cast<const float*>(ws + WS_PO) + (size_t)head * NSPLIT * DH;
  if (t < 128) {
    float M = -INFINITY;
#pragma unroll
    for (int s = 0; s < NSPLIT; ++s) M = fmaxf(M, pml[2 * s]);
    float L = 0.f, o = 0.f;
#pragma unroll
    for (int s = 0; s < NSPLIT; ++s) { const float w = exp2f(pml[2 * s] - M); L = fmaf(w, pml[2 * s + 1], L); o = fmaf(w, po[s * DH + t], o); }
    os[t] = rbf(o / L);
  }
  __syncthreads();
  float acc[8];
#pragma unroll
  for (int k = 0; k < 8; ++k) acc[k] = 0.f;
#pragma unroll
  for (int i = 0; i < 16; ++i) {
    const float hv = os[rg * 16 + i];
#pragma unroll
    for (int k = 0; k < 4; ++k) { acc[2 * k] = fmaf(hv, bflo(wv[i][k]), acc[2 * k]); acc[2 * k + 1] = fmaf(hv, bfhi(wv[i][k]), acc[2 * k + 1]); }
  }
#pragma unroll
  for (int k = 0; k < 8; ++k) yred[rg][c * 8 + k] = acc[k];
  __syncthreads();
  float y = 0.f;
#pragma unroll
  for (int g = 0; g < 8; ++g) y += yred[g][t];
  float* yo = reinterpret_cast<float*>(ws + WS_Y) + head * 1152;
  yo[t] = y; yo[576 + t] = gsum;
  __threadfence();
  __syncthreads();
  if (t == 0) {
    const unsigned old = __hip_atomic_fetch_add(reinterpret_cast<unsigned*>(ws + WS_CNT), 1u, __ATOMIC_RELAXED, __HIP_MEMORY_SCOPE_AGENT);
    lastf = (old == 31u);
  }
  __syncthreads();
  if (!lastf) return;
  __threadfence();
  const float* ya = reinterpret_cast<const float*>(ws + WS_Y);
  float s = 0.f, g = 0.f;
#pragma unroll
  for (int h = 0; h < 32; ++h) { s += __builtin_nontemporal_load(ya + h * 1152 + t); g += __builtin_nontemporal_load(ya + h * 1152 + 576 + t); }
  s = rbf(s); g = rbf(g);
  const float sg = rbf(g / (1.f + __expf(-g)));
  out[t] = __float2bfloat16(bf2f(x[t]) + rbf(s * sg));
}

extern "C" void kernel_launch(void* const* d_in, const int* in_sizes, int n_in, void* d_out, int out_size, void* d_ws, size_t ws_size,
                              hipStream_t stream) {
  const unsigned short* x = (const unsigned short*)d_in[0];
  const unsigned short* kc = (const unsigned short*)d_in[1];
  const unsigned short* vc = (const unsigned short*)d_in[2];
  const unsigned short* wq = (const unsigned short*)d_in[3];
  const unsigned short* wo = (const unsigned short*)d_in[4];
  const unsigned short* wg = (const unsigned short*)d_in[6];
  const unsigned short* wu = (const unsigned short*)d_in[7];
  const unsigned short* wd = (const unsigned short*)d_in[8];
  unsigned char* ws = (unsigned char*)d_ws;
  k_main<<<NATT + NGLU, 512, 0, stream>>>(x, kc, vc, wq, wg, wu, wd, ws);
  k_final<<<32, 576, 0, stream>>>(x, wo, ws, (__hip_bfloat16*)d_out);
}
```

```cpp
#include <hip/hip_runtime.h>
#include <hip/hip_bf16.h>
#include <stdint.h>

typedef __attribute__((ext_vector_type(4))) unsigned u32x4;
typedef __attribute__((ext_vector_type(4))) float f32x4;
typedef __attribute__((ext_vector_type(2))) __bf16 bf16x2;

constexpr int DM = 576, HQ = 32, HKV = 16, SK = 65536, DH = 128, DF = 4608, NQ = HQ * DH;
constexpr int NSPLIT = 32, WAVES = 4, KPS = SK / NSPLIT;
constexpr int PPW = KPS / 16 / WAVES;
constexpr int NATT = HKV * NSPLIT;
constexpr int NGLU = 72;

constexpr size_t WS_CNT = 0;
constexpr size_t WS_Q = 4096;
constexpr size_t WS_GLU = 16384;
constexpr size_t WS_PML = WS_GLU + 72 * 576 * 4;
constexpr size_t WS_PO = WS_PML + HQ * NSPLIT * 2 * 4;
constexpr size_t WS_Y = WS_PO + (size_t)HQ * NSPLIT * DH * 4;

__device__ __forceinline__ float bf2f(unsigned short v) { return __uint_as_float(((unsigned)v) << 16); }
__device__ __forceinline__ float bflo(unsigned v) { return __uint_as_float(v << 16); }
__device__ __forceinline__ float bfhi(unsigned v) { return __uint_as_float(v & 0xffff0000u); }
__device__ __forceinline__ float rbf(float x) { return __bfloat162float(__float2bfloat16(x)); }
__device__ __forceinline__ float dot2(unsigned k, unsigned q, float acc) {
  return __builtin_amdgcn_fdot2_f32_bf16(__builtin_bit_cast(bf16x2, k), __builtin_bit_cast(bf16x2, q), acc, false);
}
template <int CTRL> __device__ __forceinline__ float dpp(float x) {
  return __builtin_bit_cast(float, __builtin_amdgcn_mov_dpp(__builtin_bit_cast(int, x), CTRL, 0xf, 0xf, true));
}
constexpr int XOR1 = 0xB1, XOR2 = 0x4E, XOR7 = 0x141, XOR8 = 0x128, ROR4 = 0x124;
__device__ __forceinline__ float xrow16_max(float x) {
  auto s = __builtin_amdgcn_permlane16_swap(__float_as_uint(x), __float_as_uint(x), false, false);
  x = fmaxf(__uint_as_float(s[0]), __uint_as_float(s[1]));
  auto t = __builtin_amdgcn_permlane32_swap(__float_as_uint(x), __float_as_uint(x), false, false);
  return fmaxf(__uint_as_float(t[0]), __uint_as_float(t[1]));
}
__device__ __forceinline__ float xrow16_sum(float x) {
  auto s = __builtin_amdgcn_permlane16_swap(__float_as_uint(x), __float_as_uint(x), false, false);
  x = __uint_as_float(s[0]) + __uint_as_float(s[1]);
  auto t = __builtin_amdgcn_permlane32_swap(__float_as_uint(x), __float_as_uint(x), false, false);
  return __uint_as_float(t[0]) + __uint_as_float(t[1]);
}
__device__ __forceinline__ float wave_sum(float s) {
#pragma unroll
  for (int o = 32; o > 0; o >>= 1) s += __shfl_xor(s, o);
  return s;
}

__device__ __forceinline__ void layernorm_wave(const unsigned short* x, float* xs, int lane) {
  float v[9];
#pragma unroll
  for (int j = 0; j < 9; ++j) v[j] = bf2f(x[lane + 64 * j]);
  float s = 0.f;
#pragma unroll
  for (int j = 0; j < 9; ++j) s += v[j];
  s = wave_sum(s);
  const float mean = s * (1.f / DM);
  float q = 0.f;
#pragma unroll
  for (int j = 0; j < 9; ++j) { const float d = v[j] - mean; q += d * d; }
  q = wave_sum(q);
  const float var = rbf(q * (1.f / DM));
  const float mb = rbf(mean);
  const float sd = rbf(sqrtf(rbf(var + 1e-5f)));
#pragma unroll
  for (int j = 0; j < 9; ++j) xs[lane + 64 * j] = rbf(rbf(v[j] - mb) / sd);
}


__global__ void __launch_bounds__(512) k_prep(const unsigned short* __restrict__ x, const unsigned short* __restrict__ wq,
                                              const unsigned short* __restrict__ wg, const unsigned short* __restrict__ wu,
                                              const unsigned short* __restrict__ wd, unsigned char* __restrict__ ws) {
  __shared__ __attribute__((aligned(16))) float lds[7 * 576 + 8 * 128 + 64];
  float* xs = lds; float* part = lds + 7 * 576; float* hs = part + 8 * 128;
  const int tid = threadIdx.x, lane = tid & 63, wave = __builtin_amdgcn_readfirstlane(tid >> 6);
  const int rg = lane >> 3, cc = lane & 7;
  const bool isq = blockIdx.x < 64;
  const int ch = isq ? blockIdx.x : blockIdx.x - 64;
  if (blockIdx.x == 0 && tid == 0) *reinterpret_cast<unsigned*>(ws + WS_CNT) = 0u;
  const int nc = isq ? NQ : DF;
  const unsigned short* wa = isq ? wq : wg;
  const size_t off = (size_t)(wave * 72 + rg) * nc + ch * 64 + cc * 8;
  u32x4 vg[9], vu[9];
#pragma unroll
  for (int i = 0; i < 9; ++i) vg[i] = __builtin_nontemporal_load(reinterpret_cast<const u32x4*>(wa + off + (size_t)i * 8 * nc));
  if (!isq) {
#pragma unroll
    for (int i = 0; i < 9; ++i) vu[i] = __builtin_nontemporal_load(reinterpret_cast<const u32x4*>(wu + off + (size_t)i * 8 * nc));
  } else {
#pragma unroll
    for (int i = 0; i < 9; ++i) vu[i] = u32x4{0u, 0u, 0u, 0u};
  }
  if (wave == 0) layernorm_wave(x, xs, lane);
  __syncthreads();
  float ag[8], au[8];
#pragma unroll
  for (int k = 0; k < 8; ++k) { ag[k] = 0.f; au[k] = 0.f; }
#pragma unroll
  for (int i = 0; i < 9; ++i) {
    const float xv = xs[wave * 72 + i * 8 + rg];
#pragma unroll
    for (int k = 0; k < 4; ++k) {
      ag[2 * k] = fmaf(xv, bflo(vg[i][k]), ag[2 * k]); ag[2 * k + 1] = fmaf(xv, bfhi(vg[i][k]), ag[2 * k + 1]);
      au[2 * k] = fmaf(xv, bflo(vu[i][k]), au[2 * k]); au[2 * k + 1] = fmaf(xv, bfhi(vu[i][k]), au[2 * k + 1]);
    }
  }
  const int dc = tid % 72, dr = tid / 72;
  u32x4 vd[10];
  if (!isq && tid < 504) {
#pragma unroll
    for (int i = 0; i < 10; ++i) { const int r = dr + 7 * i; vd[i] = (r < 64) ? __builtin_nontemporal_load(reinterpret_cast<const u32x4*>(wd + (size_t)(ch * 64 + r) * DM + dc * 8)) : u32x4{0u, 0u, 0u, 0u}; }
  }
#pragma unroll
  for (int k = 0; k < 8; ++k) {
    float a = ag[k]; a += __shfl_xor(a, 8); a += __shfl_xor(a, 16); a += __shfl_xor(a, 32); ag[k] = a;
    float b = au[k]; b += __shfl_xor(b, 8); b += __shfl_xor(b, 16); b += __shfl_xor(b, 32); au[k] = b;
  }
  if (rg == 0) {
#pragma unroll
    for (int k = 0; k < 8; ++k) { part[wave * 128 + cc * 8 + k] = ag[k]; part[wave * 128 + 64 + cc * 8 + k] = au[k]; }
  }
  __syncthreads();
  if (tid < 64) {
    float g = 0.f, u = 0.f;
#pragma unroll
    for (int w = 0; w < 8; ++w) { g += part[w * 128 + tid]; u += part[w * 128 + 64 + tid]; }
    if (isq) { reinterpret_cast<__hip_bfloat16*>(ws + WS_Q)[ch * 64 + tid] = __float2bfloat16(g); }
    else {
      g = rbf(g); u = rbf(u);
      const float ge = 0.5f * g * (1.f + tanhf(0.7978845608028654f * (g + 0.044715f * g * g * g)));
      hs[tid] = rbf(rbf(ge) * u);
    }
  }
  if (isq) return;
  __syncthreads();
  float acc[8];
#pragma unroll
  for (int k = 0; k < 8; ++k) acc[k] = 0.f;
  if (tid < 504) {
#pragma unroll
    for (int i = 0; i < 10; ++i) { const int r = dr + 7 * i; const float hv = (r < 64) ? hs[r] : 0.f;
#pragma unroll
      for (int k = 0; k < 4; ++k) { acc[2 * k] = fmaf(hv, bflo(vd[i][k]), acc[2 * k]); acc[2 * k + 1] = fmaf(hv, bfhi(vd[i][k]), acc[2 * k + 1]); } }
#pragma unroll
    for (int k = 0; k < 8; ++k) lds[dr * 576 + dc * 8 + k] = acc[k];
  }
  __syncthreads();
  float* yo = reinterpret_cast<float*>(ws + WS_GLU) + ch * 576;
  for (int i = tid; i < 576; i += 512) {
    float sm = 0.f;
#pragma unroll
    for (int r = 0; r < 7; ++r) sm += lds[r * 576 + i];
    yo[i] = sm;
  }
}
__global__ void __launch_bounds__(256, 4) k_main(const unsigned short* __restrict__ x, const unsigned short* __restrict__ kc,
                                                 const unsigned short* __restrict__ vc, const unsigned short* __restrict__ wq,
                                                 const unsigned short* __restrict__ wg, const unsigned short* __restrict__ wu,
                                                 const unsigned short* __restrict__ wd, unsigned char* __restrict__ ws) {
  __shared__ __attribute__((aligned(16))) float lds[3 * 576 + 4 * 256 + 4 * 64 + 4 * 2 * 132];
  float* xs = lds;
  float* part = lds + 3 * 576;
  float* pex = part + 4 * 256;
  float* mrg = pex + 4 * 64;
  const int tid = threadIdx.x, lane = tid & 63, wave = __builtin_amdgcn_readfirstlane(tid >> 6);

  if (blockIdx.x >= NATT) {
    const int ch = blockIdx.x - NATT;
    const int rg = lane >> 3, cc = lane & 7;
    const int dc = tid % 72, dr = tid / 72;
    const size_t off = (size_t)(wave * 144 + rg) * DF + ch * 64 + cc * 8;
    if (wave == 0) layernorm_wave(x, xs, lane);
    __syncthreads();
    float ag[8], au[8];
#pragma unroll
    for (int k = 0; k < 8; ++k) { ag[k] = 0.f; au[k] = 0.f; }
#pragma unroll 1
    for (int hb = 0; hb < 2; ++hb) {
      u32x4 vg[9], vu[9];
#pragma unroll
      for (int i = 0; i < 9; ++i) {
        vg[i] = __builtin_nontemporal_load(reinterpret_cast<const u32x4*>(wg + off + (size_t)(hb * 9 + i) * 8 * DF));
        vu[i] = __builtin_nontemporal_load(reinterpret_cast<const u32x4*>(wu + off + (size_t)(hb * 9 + i) * 8 * DF));
      }
#pragma unroll
      for (int i = 0; i < 9; ++i) {
        const float xv = xs[wave * 144 + (hb * 9 + i) * 8 + rg];
#pragma unroll
        for (int k = 0; k < 4; ++k) {
          ag[2 * k] = fmaf(xv, bflo(vg[i][k]), ag[2 * k]); ag[2 * k + 1] = fmaf(xv, bfhi(vg[i][k]), ag[2 * k + 1]);
          au[2 * k] = fmaf(xv, bflo(vu[i][k]), au[2 * k]); au[2 * k + 1] = fmaf(xv, bfhi(vu[i][k]), au[2 * k + 1]);
        }
      }
    }
#pragma unroll
    for (int k = 0; k < 8; ++k) {
      float a = ag[k]; a += __shfl_xor(a, 8); a += __shfl_xor(a, 16); a += __shfl_xor(a, 32); ag[k] = a;
      float b = au[k]; b += __shfl_xor(b, 8); b += __shfl_xor(b, 16); b += __shfl_xor(b, 32); au[k] = b;
    }
    if (rg == 0) {
#pragma unroll
      for (int k = 0; k < 8; ++k) { part[wave * 128 + cc * 8 + k] = ag[k]; part[wave * 128 + 64 + cc * 8 + k] = au[k]; }
    }
    __syncthreads();
    float* hs = pex;
    if (tid < 64) {
      float g = 0.f, u = 0.f;
#pragma unroll
      for (int w = 0; w < 4; ++w) { g += part[w * 128 + tid]; u += part[w * 128 + 64 + tid]; }
      g = rbf(g); u = rbf(u);
      const float ge = 0.5f * g * (1.f + tanhf(0.7978845608028654f * (g + 0.044715f * g * g * g)));
      hs[tid] = rbf(rbf(ge) * u);
    }
    __syncthreads();
    float acc[8];
#pragma unroll
    for (int k = 0; k < 8; ++k) acc[k] = 0.f;
    if (tid < 216) {
#pragma unroll 11
      for (int i = 0; i < 22; ++i) { const int r = dr + 3 * i; if (r < 64) { const float hv = hs[r];
        const u32x4 v = __builtin_nontemporal_load(reinterpret_cast<const u32x4*>(wd + (size_t)(ch * 64 + r) * DM + dc * 8));
#pragma unroll
        for (int k = 0; k < 4; ++k) { acc[2 * k] = fmaf(hv, bflo(v[k]), acc[2 * k]); acc[2 * k + 1] = fmaf(hv, bfhi(v[k]), acc[2 * k + 1]); } } }
    }
    __syncthreads();
    float* yr = lds;
    if (tid < 216) {
#pragma unroll
      for (int k = 0; k < 8; ++k) yr[dr * 576 + dc * 8 + k] = acc[k];
    }
    __syncthreads();
    float* yo = reinterpret_cast<float*>(ws + WS_GLU) + ch * 576;
    for (int i = tid; i < 576; i += 256) {
      float s = 0.f;
#pragma unroll
      for (int r = 0; r < 3; ++r) s += yr[r * 576 + i];
      yo[i] = s;
    }
    return;
  }

  const int kvh = blockIdx.x & 15, sp = blockIdx.x >> 4;
  const int r = lane >> 4, c = lane & 15;
  const size_t base = ((size_t)kvh * SK + (size_t)sp * KPS + (size_t)wave * 16 + r) * DH + 8 * c;
  const unsigned short* kp = kc + base;
  const unsigned short* vp = vc + base;
  u32x4 Kn[4], Vn[4];
#define ISSUE(i_) do { _Pragma("unroll") for (int t_ = 0; t_ < 4; ++t_) { \
    Kn[t_] = __builtin_nontemporal_load(reinterpret_cast<const u32x4*>(kp + ((size_t)(i_) * (16 * WAVES) + t_ * 4) * DH)); \
    Vn[t_] = __builtin_nontemporal_load(reinterpret_cast<const u32x4*>(vp + ((size_t)(i_) * (16 * WAVES) + t_ * 4) * DH)); } } while (0)
  ISSUE(0);
  const unsigned short* qb = reinterpret_cast<const unsigned short*>(ws + WS_Q) + kvh * 256 + 8 * c;
  const u32x4 q0 = *reinterpret_cast<const u32x4*>(qb), q1 = *reinterpret_cast<const u32x4*>(qb + 128);
  constexpr float SC = 0.088388347648318440f, L2E = 1.4426950408889634f;
  float m = -INFINITY, l = 0.f, acc[16];
#pragma unroll
  for (int k = 0; k < 16; ++k) acc[k] = 0.f;
  for (int i = 0; i < PPW; ++i) {
    u32x4 Kc[4], Vc[4];
#pragma unroll
    for (int t = 0; t < 4; ++t) { Kc[t] = Kn[t]; Vc[t] = Vn[t]; }
    if (i + 1 < PPW) ISSUE(i + 1);
    float xx[8];
#pragma unroll
    for (int t = 0; t < 4; ++t) {
      float d0 = 0.f, d1 = 0.f;
#pragma unroll
      for (int k = 0; k < 4; ++k) { d0 = dot2(Kc[t][k], q0[k], d0); d1 = dot2(Kc[t][k], q1[k], d1); }
      xx[2 * t] = d0; xx[2 * t + 1] = d1;
    }
#define TR_STEP(HALF, CTRL, BIT) _Pragma("unroll") for (int h = 0; h < (HALF); ++h) { \
      const float lo_ = xx[h], hi_ = xx[h + (HALF)]; const float t1_ = lo_ + dpp<CTRL>(lo_), t2_ = hi_ + dpp<CTRL>(hi_); \
      xx[h] = (c & (BIT)) ? t2_ : t1_; }
    TR_STEP(4, XOR8, 8) TR_STEP(2, XOR7, 4) TR_STEP(1, XOR2, 2)
#undef TR_STEP
    float sc = xx[0] + dpp<XOR1>(xx[0]);
    sc = rbf(rbf(sc) * SC) * L2E;
    float pm = sc; pm = fmaxf(pm, dpp<ROR4>(pm)); pm = fmaxf(pm, dpp<XOR8>(pm)); pm = xrow16_max(pm);
    const float mn = fmaxf(m, pm);
    const float alpha = __builtin_amdgcn_exp2f(m - mn), p = __builtin_amdgcn_exp2f(sc - mn);
    l = fmaf(l, alpha, p); m = mn;
    const float a0 = dpp<0x00>(alpha), a1 = dpp<0xAA>(alpha);
#pragma unroll
    for (int k = 0; k < 8; ++k) { acc[k] *= a0; acc[8 + k] *= a1; }
    float* prow = pex + (wave * 4 + r) * 16;
    prow[c] = p;
    asm volatile("s_waitcnt lgkmcnt(0)" ::: "memory");
    f32x4 pr[4];
#pragma unroll
    for (int u = 0; u < 4; ++u) pr[u] = *reinterpret_cast<const f32x4*>(prow + 4 * u);
#pragma unroll
    for (int t = 0; t < 4; ++t) {
      const float p0 = pr[t][0], p1 = pr[t][2];
#pragma unroll
      for (int k = 0; k < 4; ++k) {
        const float lo = bflo(Vc[t][k]), hi = bfhi(Vc[t][k]);
        acc[2 * k] = fmaf(p0, lo, acc[2 * k]); acc[2 * k + 1] = fmaf(p0, hi, acc[2 * k + 1]);
        acc[8 + 2 * k] = fmaf(p1, lo, acc[8 + 2 * k]); acc[8 + 2 * k + 1] = fmaf(p1, hi, acc[8 + 2 * k + 1]);
      }
    }
  }
#undef ISSUE
#pragma unroll
  for (int k = 0; k < 16; ++k) acc[k] = xrow16_sum(acc[k]);
  { float t = l; t += dpp<ROR4>(t); t += dpp<XOR8>(t); l = xrow16_sum(t); }
  if (r == 0) {
#pragma unroll
    for (int g = 0; g < 2; ++g) {
      float* rec = mrg + (wave * 2 + g) * 132;
#pragma unroll
      for (int k = 0; k < 8; k += 4) *reinterpret_cast<f32x4*>(rec + 4 + 8 * c + k) = f32x4{acc[g * 8 + k], acc[g * 8 + k + 1], acc[g * 8 + k + 2], acc[g * 8 + k + 3]};
      if (c == 2 * g) { rec[0] = m; rec[1] = l; }
    }
  }
  __syncthreads();
  if (wave < 2) {
    const int g = wave, d0 = 2 * lane;
    float M = -INFINITY;
#pragma unroll
    for (int w = 0; w < WAVES; ++w) M = fmaxf(M, mrg[(w * 2 + g) * 132]);
    float L = 0.f, o0 = 0.f, o1 = 0.f;
#pragma unroll
    for (int w = 0; w < WAVES; ++w) { const float* rec = mrg + (w * 2 + g) * 132;
      const float wt = __builtin_amdgcn_exp2f(rec[0] - M);
      L = fmaf(wt, rec[1], L); o0 = fmaf(wt, rec[4 + d0], o0); o1 = fmaf(wt, rec[4 + d0 + 1], o1); }
    const int head = kvh * 2 + g;
    float* pml = reinterpret_cast<float*>(ws + WS_PML) + ((size_t)head * NSPLIT + sp) * 2;
    float* po = reinterpret_cast<float*>(ws + WS_PO) + ((size_t)head * NSPLIT + sp) * DH;
    *reinterpret_cast<float2*>(po + d0) = make_float2(o0, o1);
    if (lane == 0) { pml[0] = M; pml[1] = L; }
  }
}

__global__ void __launch_bounds__(576) k_final(const unsigned short* __restrict__ x, const unsigned short* __restrict__ wo,
                                               unsigned char* __restrict__ ws, __hip_bfloat16* __restrict__ out) {
  __shared__ float os[128];
  __shared__ float yred[8][576];
  __shared__ unsigned lastf;
  const int head = blockIdx.x, t = threadIdx.x;
  const int c = t % 72, rg = t / 72;
  u32x4 wv[16];
#pragma unroll
  for (int i = 0; i < 16; ++i) wv[i] = __builtin_nontemporal_load(reinterpret_cast<const u32x4*>(wo + (size_t)(head * 128 + rg * 16 + i) * DM + c * 8));
  const float* gl = reinterpret_cast<const float*>(ws + WS_GLU);
  float gsum = gl[head * 576 + t] + gl[(head + 32) * 576 + t] + (head < 8 ? gl[(head + 64) * 576 + t] : 0.f);
  const float* pml = reinterpret_cast<const float*>(ws + WS_PML) + (size_t)head * NSPLIT * 2;
  const float* po = reinterpret_cast<const float*>(ws + WS_PO) + (size_t)head * NSPLIT * DH;
  if (t < 128) {
    float M = -INFINITY;
#pragma unroll
    for (int s = 0; s < NSPLIT; ++s) M = fmaxf(M, pml[2 * s]);
    float L = 0.f, o = 0.f;
#pragma unroll
    for (int s = 0; s < NSPLIT; ++s) { const float w = exp2f(pml[2 * s] - M); L = fmaf(w, pml[2 * s + 1], L); o = fmaf(w, po[s * DH + t], o); }
    os[t] = rbf(o / L);
  }
  __syncthreads();
  float acc[8];
#pragma unroll
  for (int k = 0; k < 8; ++k) acc[k] = 0.f;
#pragma unroll
  for (int i = 0; i < 16; ++i) {
    const float hv = os[rg * 16 + i];
#pragma unroll
    for (int k = 0; k < 4; ++k) { acc[2 * k] = fmaf(hv, bflo(wv[i][k]), acc[2 * k]); acc[2 * k + 1] = fmaf(hv, bfhi(wv[i][k]), acc[2 * k + 1]); }
  }
#pragma unroll
  for (int k = 0; k < 8; ++k) yred[rg][c * 8 + k] = acc[k];
  __syncthreads();
  float y = 0.f;
#pragma unroll
  for (int g = 0; g < 8; ++g) y += yred[g][t];
  float* yo = reinterpret_cast<float*>(ws + WS_Y) + head * 1152;
  yo[t] = y; yo[576 + t] = gsum;
  asm volatile("s_waitcnt vmcnt(0)" ::: "memory");
  __syncthreads();
  if (t == 0) {
    __builtin_amdgcn_fence(__ATOMIC_RELEASE, "agent");
    asm volatile("s_waitcnt vmcnt(0)" ::: "memory");
    const unsigned old = __hip_atomic_fetch_add(reinterpret_cast<unsigned*>(ws + WS_CNT), 1u, __ATOMIC_RELAXED, __HIP_MEMORY_SCOPE_AGENT);
    lastf = (old == 31u);
    if (old == 31u) { __builtin_amdgcn_fence(__ATOMIC_ACQUIRE, "agent"); asm volatile("s_waitcnt vmcnt(0)" ::: "memory"); }
  }
  __syncthreads();
  if (!lastf) return;
  const float* ya = reinterpret_cast<const float*>(ws + WS_Y);
  float s = 0.f, g = 0.f;
#pragma unroll
  for (int h = 0; h < 32; ++h) { s += __builtin_nontemporal_load(ya + h * 1152 + t); g += __builtin_nontemporal_load(ya + h * 1152 + 576 + t); }
  s = rbf(s); g = rbf(g);
  const float sg = rbf(g / (1.f + __expf(-g)));
  out[t] = __float2bfloat16(bf2f(x[t]) + rbf(s * sg));
}

extern "C" void kernel_launch(void* const* d_in, const int* in_sizes, int n_in, void* d_out, int out_size, void* d_ws, size_t ws_size,
                              hipStream_t stream) {
  const unsigned short* x = (const unsigned short*)d_in[0];
  const unsigned short* kc = (const unsigned short*)d_in[1];
  const unsigned short* vc = (const unsigned short*)d_in[2];
  const unsigned short* wq = (const unsigned short*)d_in[3];
  const unsigned short* wo = (const unsigned short*)d_in[4];
  const unsigned short* wg = (const unsigned short*)d_in[6];
  const unsigned short* wu = (const unsigned short*)d_in[7];
  const unsigned short* wd = (const unsigned short*)d_in[8];
  unsigned char* ws = (unsigned char*)d_ws;
  k_prep<<<64, 512, 0, stream>>>(x, wq, wg, wu, wd, ws);
  k_main<<<NATT + NGLU, 256, 0, stream>>>(x, kc, vc, wq, wg, wu, wd, ws);
  k_final<<<32, 576, 0, stream>>>(x, wo, ws, (__hip_bfloat16*)d_out);
}
```

```cpp
#include <hip/hip_runtime.h>
#include <hip/hip_bf16.h>
#include <stdint.h>

typedef __attribute__((ext_vector_type(4))) unsigned u32x4;
typedef __attribute__((ext_vector_type(4))) float f32x4;
typedef __attribute__((ext_vector_type(2))) __bf16 bf16x2;

constexpr int DM = 576, HQ = 32, HKV = 16, SK = 65536, DH = 128, DF = 4608, NQ = HQ * DH;
constexpr int NSPLIT = 32, WAVES = 4, KPS = SK / NSPLIT;
constexpr int PPW = KPS / 16 / WAVES;
constexpr int NATT = HKV * NSPLIT;
constexpr int NGLU = 72;

constexpr size_t WS_CNT = 0;
constexpr size_t WS_Q = 4096;
constexpr size_t WS_GLU = 16384;
constexpr size_t WS_PML = WS_GLU + 72 * 576 * 4;
constexpr size_t WS_PO = WS_PML + HQ * NSPLIT * 2 * 4;
constexpr size_t WS_Y = WS_PO + (size_t)HQ * NSPLIT * DH * 4;

__device__ __forceinline__ float bf2f(unsigned short v) { return __uint_as_float(((unsigned)v) << 16); }
__device__ __forceinline__ float bflo(unsigned v) { return __uint_as_float(v << 16); }
__device__ __forceinline__ float bfhi(unsigned v) { return __uint_as_float(v & 0xffff0000u); }
__device__ __forceinline__ float rbf(float x) { return __bfloat162float(__float2bfloat16(x)); }
__device__ __forceinline__ float dot2(unsigned k, unsigned q, float acc) {
  return __builtin_amdgcn_fdot2_f32_bf16(__builtin_bit_cast(bf16x2, k), __builtin_bit_cast(bf16x2, q), acc, false);
}
template <int CTRL> __device__ __forceinline__ float dpp(float x) {
  return __builtin_bit_cast(float, __builtin_amdgcn_mov_dpp(__builtin_bit_cast(int, x), CTRL, 0xf, 0xf, true));
}
constexpr int XOR1 = 0xB1, XOR2 = 0x4E, XOR7 = 0x141, XOR8 = 0x128, ROR4 = 0x124;
__device__ __forceinline__ float xrow16_max(float x) {
  auto s = __builtin_amdgcn_permlane16_swap(__float_as_uint(x), __float_as_uint(x), false, false);
  x = fmaxf(__uint_as_float(s[0]), __uint_as_float(s[1]));
  auto t = __builtin_amdgcn_permlane32_swap(__float_as_uint(x), __float_as_uint(x), false, false);
  return fmaxf(__uint_as_float(t[0]), __uint_as_float(t[1]));
}
__device__ __forceinline__ float xrow16_sum(float x) {
  auto s = __builtin_amdgcn_permlane16_swap(__float_as_uint(x), __float_as_uint(x), false, false);
  x = __uint_as_float(s[0]) + __uint_as_float(s[1]);
  auto t = __builtin_amdgcn_permlane32_swap(__float_as_uint(x), __float_as_uint(x), false, false);
  return __uint_as_float(t[0]) + __uint_as_float(t[1]);
}
__device__ __forceinline__ float wave_sum(float s) {
#pragma unroll
  for (int o = 32; o > 0; o >>= 1) s += __shfl_xor(s, o);
  return s;
}

__device__ __forceinline__ void layernorm_wave(const unsigned short* x, float* xs, int lane) {
  float v[9];
#pragma unroll
  for (int j = 0; j < 9; ++j) v[j] = bf2f(x[lane + 64 * j]);
  float s = 0.f;
#pragma unroll
  for (int j = 0; j < 9; ++j) s += v[j];
  s = wave_sum(s);
  const float mean = s * (1.f / DM);
  float q = 0.f;
#pragma unroll
  for (int j = 0; j < 9; ++j) { const float d = v[j] - mean; q += d * d; }
  q = wave_sum(q);
  const float var = rbf(q * (1.f / DM));
  const float mb = rbf(mean);
  const float sd = rbf(sqrtf(rbf(var + 1e-5f)));
#pragma unroll
  for (int j = 0; j < 9; ++j) xs[lane + 64 * j] = rbf(rbf(v[j] - mb) / sd);
}


__global__ void __launch_bounds__(512) k_prep(const unsigned short* __restrict__ x, const unsigned short* __restrict__ wq,
                                              const unsigned short* __restrict__ wg, const unsigned short* __restrict__ wu,
                                              const unsigned short* __restrict__ wd, unsigned char* __restrict__ ws) {
  __shared__ __attribute__((aligned(16))) float lds[7 * 576 + 8 * 128 + 64];
  float* xs = lds; float* part = lds + 7 * 576; float* hs = part + 8 * 128;
  const int tid = threadIdx.x, lane = tid & 63, wave = __builtin_amdgcn_readfirstlane(tid >> 6);
  const int rg = lane >> 3, cc = lane & 7;
  const bool isq = blockIdx.x < 64;
  const int ch = isq ? blockIdx.x : blockIdx.x - 64;
  if (blockIdx.x == 0 && tid == 0) *reinterpret_cast<unsigned*>(ws + WS_CNT) = 0u;
  const int nc = isq ? NQ : DF;
  const unsigned short* wa = isq ? wq : wg;
  const size_t off = (size_t)(wave * 72 + rg) * nc + ch * 64 + cc * 8;
  u32x4 vg[9], vu[9];
#pragma unroll
  for (int i = 0; i < 9; ++i) vg[i] = __builtin_nontemporal_load(reinterpret_cast<const u32x4*>(wa + off + (size_t)i * 8 * nc));
  if (!isq) {
#pragma unroll
    for (int i = 0; i < 9; ++i) vu[i] = __builtin_nontemporal_load(reinterpret_cast<const u32x4*>(wu + off + (size_t)i * 8 * nc));
  } else {
#pragma unroll
    for (int i = 0; i < 9; ++i) vu[i] = u32x4{0u, 0u, 0u, 0u};
  }
  if (wave == 0) layernorm_wave(x, xs, lane);
  __syncthreads();
  float ag[8], au[8];
#pragma unroll
  for (int k = 0; k < 8; ++k) { ag[k] = 0.f; au[k] = 0.f; }
#pragma unroll
  for (int i = 0; i < 9; ++i) {
    const float xv = xs[wave * 72 + i * 8 + rg];
#pragma unroll
    for (int k = 0; k < 4; ++k) {
      ag[2 * k] = fmaf(xv, bflo(vg[i][k]), ag[2 * k]); ag[2 * k + 1] = fmaf(xv, bfhi(vg[i][k]), ag[2 * k + 1]);
      au[2 * k] = fmaf(xv, bflo(vu[i][k]), au[2 * k]); au[2 * k + 1] = fmaf(xv, bfhi(vu[i][k]), au[2 * k + 1]);
    }
  }
  const int dc = tid % 72, dr = tid / 72;
  u32x4 vd[10];
  if (!isq && tid < 504) {
#pragma unroll
    for (int i = 0; i < 10; ++i) { const int r = dr + 7 * i; vd[i] = (r < 64) ? __builtin_nontemporal_load(reinterpret_cast<const u32x4*>(wd + (size_t)(ch * 64 + r) * DM + dc * 8)) : u32x4{0u, 0u, 0u, 0u}; }
  }
#pragma unroll
  for (int k = 0; k < 8; ++k) {
    float a = ag[k]; a += __shfl_xor(a, 8); a += __shfl_xor(a, 16); a += __shfl_xor(a, 32); ag[k] = a;
    float b = au[k]; b += __shfl_xor(b, 8); b += __shfl_xor(b, 16); b += __shfl_xor(b, 32); au[k] = b;
  }
  if (rg == 0) {
#pragma unroll
    for (int k = 0; k < 8; ++k) { part[wave * 128 + cc * 8 + k] = ag[k]; part[wave * 128 + 64 + cc * 8 + k] = au[k]; }
  }
  __syncthreads();
  if (tid < 64) {
    float g = 0.f, u = 0.f;
#pragma unroll
    for (int w = 0; w < 8; ++w) { g += part[w * 128 + tid]; u += part[w * 128 + 64 + tid]; }
    if (isq) { reinterpret_cast<__hip_bfloat16*>(ws + WS_Q)[ch * 64 + tid] = __float2bfloat16(g); }
    else {
      g = rbf(g); u = rbf(u);
      const float ge = 0.5f * g * (1.f + tanhf(0.7978845608028654f * (g + 0.044715f * g * g * g)));
      hs[tid] = rbf(rbf(ge) * u);
    }
  }
  if (isq) return;
  __syncthreads();
  float acc[8];
#pragma unroll
  for (int k = 0; k < 8; ++k) acc[k] = 0.f;
  if (tid < 504) {
#pragma unroll
    for (int i = 0; i < 10; ++i) { const int r = dr + 7 * i; const float hv = (r < 64) ? hs[r] : 0.f;
#pragma unroll
      for (int k = 0; k < 4; ++k) { acc[2 * k] = fmaf(hv, bflo(vd[i][k]), acc[2 * k]); acc[2 * k + 1] = fmaf(hv, bfhi(vd[i][k]), acc[2 * k + 1]); } }
#pragma unroll
    for (int k = 0; k < 8; ++k) lds[dr * 576 + dc * 8 + k] = acc[k];
  }
  __syncthreads();
  float* yo = reinterpret_cast<float*>(ws + WS_GLU) + ch * 576;
  for (int i = tid; i < 576; i += 512) {
    float sm = 0.f;
#pragma unroll
    for (int r = 0; r < 7; ++r) sm += lds[r * 576 + i];
    yo[i] = sm;
  }
}
__global__ void __launch_bounds__(256, 4) k_main(const unsigned short* __restrict__ x, const unsigned short* __restrict__ kc,
                                                 const unsigned short* __restrict__ vc, const unsigned short* __restrict__ wq,
                                                 const unsigned short* __restrict__ wg, const unsigned short* __restrict__ wu,
                                                 const unsigned short* __restrict__ wd, unsigned char* __restrict__ ws) {
  __shared__ __attribute__((aligned(16))) float lds[3 * 576 + 4 * 256 + 4 * 64 + 4 * 2 * 132];
  float* xs = lds;
  float* part = lds + 3 * 576;
  float* pex = part + 4 * 256;
  float* mrg = pex + 4 * 64;
  const int tid = threadIdx.x, lane = tid & 63, wave = __builtin_amdgcn_readfirstlane(tid >> 6);

  const int kvh = blockIdx.x & 15, sp = blockIdx.x >> 4;
  const int r = lane >> 4, c = lane & 15;
  const size_t base = ((size_t)kvh * SK + (size_t)sp * KPS + (size_t)wave * 16 + r) * DH + 8 * c;
  const unsigned short* kp = kc + base;
  const unsigned short* vp = vc + base;
  u32x4 Kn[4], Vn[4];
#define ISSUE(i_) do { _Pragma("unroll") for (int t_ = 0; t_ < 4; ++t_) { \
    Kn[t_] = __builtin_nontemporal_load(reinterpret_cast<const u32x4*>(kp + ((size_t)(i_) * (16 * WAVES) + t_ * 4) * DH)); \
    Vn[t_] = __builtin_nontemporal_load(reinterpret_cast<const u32x4*>(vp + ((size_t)(i_) * (16 * WAVES) + t_ * 4) * DH)); } } while (0)
  ISSUE(0);
  const unsigned short* qb = reinterpret_cast<const unsigned short*>(ws + WS_Q) + kvh * 256 + 8 * c;
  const u32x4 q0 = *reinterpret_cast<const u32x4*>(qb), q1 = *reinterpret_cast<const u32x4*>(qb + 128);
  constexpr float SC = 0.088388347648318440f, L2E = 1.4426950408889634f;
  float m = -INFINITY, l = 0.f, acc[16];
#pragma unroll
  for (int k = 0; k < 16; ++k) acc[k] = 0.f;
  for (int i = 0; i < PPW; ++i) {
    u32x4 Kc[4], Vc[4];
#pragma unroll
    for (int t = 0; t < 4; ++t) { Kc[t] = Kn[t]; Vc[t] = Vn[t]; }
    if (i + 1 < PPW) ISSUE(i + 1);
    float xx[8];
#pragma unroll
    for (int t = 0; t < 4; ++t) {
      float d0 = 0.f, d1 = 0.f;
#pragma unroll
      for (int k = 0; k < 4; ++k) { d0 = dot2(Kc[t][k], q0[k], d0); d1 = dot2(Kc[t][k], q1[k], d1); }
      xx[2 * t] = d0; xx[2 * t + 1] = d1;
    }
#define TR_STEP(HALF, CTRL, BIT) _Pragma("unroll") for (int h = 0; h < (HALF); ++h) { \
      const float lo_ = xx[h], hi_ = xx[h + (HALF)]; const float t1_ = lo_ + dpp<CTRL>(lo_), t2_ = hi_ + dpp<CTRL>(hi_); \
      xx[h] = (c & (BIT)) ? t2_ : t1_; }
    TR_STEP(4, XOR8, 8) TR_STEP(2, XOR7, 4) TR_STEP(1, XOR2, 2)
#undef TR_STEP
    float sc = xx[0] + dpp<XOR1>(xx[0]);
    sc = rbf(rbf(sc) * SC) * L2E;
    float pm = sc; pm = fmaxf(pm, dpp<ROR4>(pm)); pm = fmaxf(pm, dpp<XOR8>(pm)); pm = xrow16_max(pm);
    const float mn = fmaxf(m, pm);
    const float alpha = __builtin_amdgcn_exp2f(m - mn), p = __builtin_amdgcn_exp2f(sc - mn);
    l = fmaf(l, alpha, p); m = mn;
    const float a0 = dpp<0x00>(alpha), a1 = dpp<0xAA>(alpha);
#pragma unroll
    for (int k = 0; k < 8; ++k) { acc[k] *= a0; acc[8 + k] *= a1; }
    float* prow = pex + (wave * 4 + r) * 16;
    prow[c] = p;
    asm volatile("s_waitcnt lgkmcnt(0)" ::: "memory");
    f32x4 pr[4];
#pragma unroll
    for (int u = 0; u < 4; ++u) pr[u] = *reinterpret_cast<const f32x4*>(prow + 4 * u);
#pragma unroll
    for (int t = 0; t < 4; ++t) {
      const float p0 = pr[t][0], p1 = pr[t][2];
#pragma unroll
      for (int k = 0; k < 4; ++k) {
        const float lo = bflo(Vc[t][k]), hi = bfhi(Vc[t][k]);
        acc[2 * k] = fmaf(p0, lo, acc[2 * k]); acc[2 * k + 1] = fmaf(p0, hi, acc[2 * k + 1]);
        acc[8 + 2 * k] = fmaf(p1, lo, acc[8 + 2 * k]); acc[8 + 2 * k + 1] = fmaf(p1, hi, acc[8 + 2 * k + 1]);
      }
    }
  }
#undef ISSUE
#pragma unroll
  for (int k = 0; k < 16; ++k) acc[k] = xrow16_sum(acc[k]);
  { float t = l; t += dpp<ROR4>(t); t += dpp<XOR8>(t); l = xrow16_sum(t); }
  if (r == 0) {
#pragma unroll
    for (int g = 0; g < 2; ++g) {
      float* rec = mrg + (wave * 2 + g) * 132;
#pragma unroll
      for (int k = 0; k < 8; k += 4) *reinterpret_cast<f32x4*>(rec + 4 + 8 * c + k) = f32x4{acc[g * 8 + k], acc[g * 8 + k + 1], acc[g * 8 + k + 2], acc[g * 8 + k + 3]};
      if (c == 2 * g) { rec[0] = m; rec[1] = l; }
    }
  }
  __syncthreads();
  if (wave < 2) {
    const int g = wave, d0 = 2 * lane;
    float M = -INFINITY;
#pragma unroll
    for (int w = 0; w < WAVES; ++w) M = fmaxf(M, mrg[(w * 2 + g) * 132]);
    float L = 0.f, o0 = 0.f, o1 = 0.f;
#pragma unroll
    for (int w = 0; w < WAVES; ++w) { const float* rec = mrg + (w * 2 + g) * 132;
      const float wt = __builtin_amdgcn_exp2f(rec[0] - M);
      L = fmaf(wt, rec[1], L); o0 = fmaf(wt, rec[4 + d0], o0); o1 = fmaf(wt, rec[4 + d0 + 1], o1); }
    const int head = kvh * 2 + g;
    float* pml = reinterpret_cast<float*>(ws + WS_PML) + ((size_t)head * NSPLIT + sp) * 2;
    float* po = reinterpret_cast<float*>(ws + WS_PO) + ((size_t)head * NSPLIT + sp) * DH;
    *reinterpret_cast<float2*>(po + d0) = make_float2(o0, o1);
    if (lane == 0) { pml[0] = M; pml[1] = L; }
  }
}

__global__ void __launch_bounds__(576) k_final(const unsigned short* __restrict__ x, const unsigned short* __restrict__ wo,
                                               unsigned char* __restrict__ ws, __hip_bfloat16* __restrict__ out) {
  __shared__ float os[128];
  __shared__ float yred[8][576];
  __shared__ unsigned lastf;
  const int head = blockIdx.x, t = threadIdx.x;
  const int c = t % 72, rg = t / 72;
  u32x4 wv[16];
#pragma unroll
  for (int i = 0; i < 16; ++i) wv[i] = __builtin_nontemporal_load(reinterpret_cast<const u32x4*>(wo + (size_t)(head * 128 + rg * 16 + i) * DM + c * 8));
  const float* gl = reinterpret_cast<const float*>(ws + WS_GLU);
  float gsum = gl[head * 576 + t] + gl[(head + 32) * 576 + t] + (head < 8 ? gl[(head + 64) * 576 + t] : 0.f);
  const float* pml = reinterpret_cast<const float*>(ws + WS_PML) + (size_t)head * NSPLIT * 2;
  const float* po = reinterpret_cast<const float*>(ws + WS_PO) + (size_t)head * NSPLIT * DH;
  if (t < 128) {
    float M = -INFINITY;
#pragma unroll
    for (int s = 0; s < NSPLIT; ++s) M = fmaxf(M, pml[2 * s]);
    float L = 0.f, o = 0.f;
#pragma unroll
    for (int s = 0; s < NSPLIT; ++s) { const float w = exp2f(pml[2 * s] - M); L = fmaf(w, pml[2 * s + 1], L); o = fmaf(w, po[s * DH + t], o); }
    os[t] = rbf(o / L);
  }
  __syncthreads();
  float acc[8];
#pragma unroll
  for (int k = 0; k < 8; ++k) acc[k] = 0.f;
#pragma unroll
  for (int i = 0; i < 16; ++i) {
    const float hv = os[rg * 16 + i];
#pragma unroll
    for (int k = 0; k < 4; ++k) { acc[2 * k] = fmaf(hv, bflo(wv[i][k]), acc[2 * k]); acc[2 * k + 1] = fmaf(hv, bfhi(wv[i][k]), acc[2 * k + 1]); }
  }
#pragma unroll
  for (int k = 0; k < 8; ++k) yred[rg][c * 8 + k] = acc[k];
  __syncthreads();
  float y = 0.f;
#pragma unroll
  for (int g = 0; g < 8; ++g) y += yred[g][t];
  float* yo = reinterpret_cast<float*>(ws + WS_Y) + head * 1152;
  yo[t] = y; yo[576 + t] = gsum;
  asm volatile("s_waitcnt vmcnt(0)" ::: "memory");
  __syncthreads();
  if (t == 0) {
    __builtin_amdgcn_fence(__ATOMIC_RELEASE, "agent");
    asm volatile("s_waitcnt vmcnt(0)" ::: "memory");
    const unsigned old = __hip_atomic_fetch_add(reinterpret_cast<unsigned*>(ws + WS_CNT), 1u, __ATOMIC_RELAXED, __HIP_MEMORY_SCOPE_AGENT);
    lastf = (old == 31u);
    if (old == 31u) { __builtin_amdgcn_fence(__ATOMIC_ACQUIRE, "agent"); asm volatile("s_waitcnt vmcnt(0)" ::: "memory"); }
  }
  __syncthreads();
  if (!lastf) return;
  const float* ya = reinterpret_cast<const float*>(ws + WS_Y);
  float s = 0.f, g = 0.f;
#pragma unroll
  for (int h = 0; h < 32; ++h) { s += __builtin_nontemporal_load(ya + h * 1152 + t); g += __builtin_nontemporal_load(ya + h * 1152 + 576 + t); }
  s = rbf(s); g = rbf(g);
  const float sg = rbf(g / (1.f + __expf(-g)));
  out[t] = __float2bfloat16(bf2f(x[t]) + rbf(s * sg));
}

extern "C" void kernel_launch(void* const* d_in, const int* in_sizes, int n_in, void* d_out, int out_size, void* d_ws, size_t ws_size,
                              hipStream_t stream) {
  const unsigned short* x = (const unsigned short*)d_in[0];
  const unsigned short* kc = (const unsigned short*)d_in[1];
  const unsigned short* vc = (const unsigned short*)d_in[2];
  const unsigned short* wq = (const unsigned short*)d_in[3];
  const unsigned short* wo = (const unsigned short*)d_in[4];
  const unsigned short* wg = (const unsigned short*)d_in[6];
  const unsigned short* wu = (const unsigned short*)d_in[7];
  const unsigned short* wd = (const unsigned short*)d_in[8];
  unsigned char* ws = (unsigned char*)d_ws;
  k_prep<<<136, 512, 0, stream>>>(x, wq, wg, wu, wd, ws);
  k_main<<<NATT, 256, 0, stream>>>(x, kc, vc, wq, wg, wu, wd, ws);
  k_final<<<32, 576, 0, stream>>>(x, wo, ws, (__hip_bfloat16*)d_out);
}
```
